```python
import torch

def _softmax(x, axis=-1):
    return torch.softmax(x, dim=axis)
def _gelu(x):
    return torch.nn.functional.gelu(x, approximate="tanh")
def _silu(x):
    return torch.nn.functional.silu(x)
def _sigmoid(x):
    return torch.sigmoid(x)
def _layernorm(x, axis=-1, eps=1e-5):
    m = x.mean(dim=axis, keepdim=True)
    v = x.var(dim=axis, keepdim=True, unbiased=False)
    return (x - m) / torch.sqrt(v + eps)
def _scatter_add(tgt, idx, src, axis=0):
    return tgt.clone().index_add_(axis, idx, src)
def _segment_sum(src, seg, n):
    out = torch.zeros((n,) + src.shape[1:], dtype=src.dtype)
    return out.index_add_(0, seg, src)
def _pad(x, pw):
    flat = tuple(v for pair in reversed(pw) for v in pair)
    return torch.nn.functional.pad(x, flat)

kernel_name = 'recurrent_scan__mha_block_cached__glu_gated_swiglu__decode'

def setup_inputs():

    _w = (lambda a: (lambda t: t.to(torch.bfloat16) if t.is_floating_point() else t)(torch.as_tensor(a)))
    def _A_setup():
        B, Sq, D = 4, 1, 512
        H, Sk, Dh = 32, 8192, 128
        return {
            "x": _w((lambda *s: torch.randn(*s, dtype=torch.bfloat16) * 0.2)(B, Sq, D)),
            "k_cache": _w((lambda *s: torch.randn(*s, dtype=torch.bfloat16) * 1.6)(B, H, Sk, Dh)),
            "v_cache": _w((lambda *s: torch.randn(*s, dtype=torch.bfloat16) * 1.6)(B, H, Sk, Dh)),
            "wq": _w((lambda *s: torch.randn(*s, dtype=torch.bfloat16) * 1.6)(D, H * Dh) * (D ** -0.5)),
            "wo": _w((lambda *s: torch.randn(*s, dtype=torch.bfloat16) * 1.6)(H * Dh, D) * ((H * Dh) ** -0.5)),
        }
    _A_in = _A_setup()

    def _B_setup():
        B, S, D, F = 4, 1, 512, 1792
        return {
            "x": _w((lambda *s: torch.randn(*s, dtype=torch.bfloat16) * 0.2)(B, S, D)),
            "w1": _w((lambda *s: torch.randn(*s, dtype=torch.bfloat16) * 0.8)(D, F) * (2.0 / D) ** 0.5),
            "w3": _w((lambda *s: torch.randn(*s, dtype=torch.bfloat16) * 0.8)(D, F) * (2.0 / D) ** 0.5),
            "w2": _w((lambda *s: torch.randn(*s, dtype=torch.bfloat16) * 0.8)(F, D) * (2.0 / F) ** 0.5),
        }
    _B_in = _B_setup()

    return {
        "A_x": _A_in["x"],
        "A_k_cache": _A_in["k_cache"],
        "A_v_cache": _A_in["v_cache"],
        "A_wq": _A_in["wq"],
        "A_wo": _A_in["wo"],
        "B_x": _B_in["x"],
        "B_w1": _B_in["w1"],
        "B_w3": _B_in["w3"],
        "B_w2": _B_in["w2"],
    }

def reference(A_x, A_k_cache, A_v_cache, A_wq, A_wo, B_x, B_w1, B_w3, B_w2):
    def _A_ref(x, k_cache, v_cache, wq, wo):
        B, Sq, D = x.shape
        H, Dh = 32, 128
        q = (lambda x, *d: x.permute(*d))((x @ wq).reshape(B, Sq, H, Dh), 0, 2, 1, 3)
        scores = torch.einsum("bhqd,bhkd->bhqk", q, k_cache) * (Dh ** -0.5)
        p = _softmax(scores, axis=-1)
        o = torch.einsum("bhqk,bhkd->bhqd", p, v_cache)
        return (lambda x, *d: x.permute(*d))(o, 0, 2, 1, 3).reshape(B, Sq, H * Dh) @ wo

    def _B_ref(x, w1, w3, w2):
        gate = torch.einsum("bsd,df->bsf", x, w1)
        up = torch.einsum("bsd,df->bsf", x, w3)
        h = _silu(gate) * up
        out = torch.einsum("bsf,fd->bsd", h, w2)
        return out

    x = A_x
    xn = _layernorm(x, axis=-1)
    s = _A_ref(xn, A_k_cache, A_v_cache, A_wq, A_wo)
    g = _silu(_B_ref(xn, B_w1, B_w3, B_w2))
    return x + s * g
```

```cpp
#define FIN_SLEEP 4
#define PBUF 48
#define HMAP 2
#define WIN_A 5
#define WIN_B 5
#define WIN_L 6
#define PACE 7420
#define LAG 100
#define LAG2 350
#define WIN_M 7
#include <hip/hip_runtime.h>
#include <hip/hip_bf16.h>
#include <stdint.h>

#define WQ_WIN 16
#define W_AUX 2
#define HW_WIN 2

#define PRIO 3
#define K_AUX 2
#define V_AUX 2
#define ST_AUX 16
#define SKEW 1
#define FFN_DT 0
using u32x4 = __attribute__((ext_vector_type(4))) unsigned;
using f32x4 = __attribute__((ext_vector_type(4))) float;
using bf16x2 = __attribute__((ext_vector_type(2))) __bf16;
typedef __attribute__((address_space(1))) unsigned gu32;
typedef __attribute__((address_space(3))) unsigned lu32;
typedef __amdgpu_buffer_rsrc_t rsrc_t;

constexpr int NB = 4, DM = 512, NH = 32, SK = 8192, DH = 128, FF = 1792;
constexpr int NSW = 4;
constexpr int RING = 8;
constexpr int PB = PBUF;
constexpr int NT = 256;
constexpr int T_FILL = ((PB + RING - 4) * 2 * PACE) >> 10;
constexpr int NFFN = 28;
constexpr unsigned WS_Y = 4096, WS_F = WS_Y + 256 * 4096, WS_END = WS_F + NFFN * 4 * 4096;
constexpr unsigned SPIN_CAP = 20000000;
constexpr float C2 = 0.088388347648318440f * 1.4426950408889634f;

#define SB __builtin_amdgcn_sched_barrier(0);
#define VMCNT(n) asm volatile("s_waitcnt vmcnt(%0)" ::"n"(n) : "memory")
#define LGKM0 asm volatile("s_waitcnt lgkmcnt(0)" ::: "memory")
#define WGBAR do { LGKM0; __builtin_amdgcn_s_barrier(); asm volatile("" ::: "memory"); } while (0)

__device__ __forceinline__ float dot2(unsigned a, unsigned b, float acc) {
  return __builtin_amdgcn_fdot2_f32_bf16(__builtin_bit_cast(bf16x2, a), __builtin_bit_cast(bf16x2, b), acc, false);
}
template <int CTRL> __device__ __forceinline__ float dpp(float x) {
  return __builtin_bit_cast(float, __builtin_amdgcn_mov_dpp(__builtin_bit_cast(int, x), CTRL, 0xf, 0xf, true));
}
template <int N> __device__ __forceinline__ unsigned bc(unsigned x) { return __builtin_amdgcn_mov_dpp(x, 0x150 + N, 0xf, 0xf, true); }
__device__ __forceinline__ float rowsum(float x) {
  x += dpp<0x128>(x); x += dpp<0x124>(x); x += dpp<0x122>(x); x += dpp<0x121>(x); return x;
}
__device__ __forceinline__ float xrow_sum(float x) {
  auto s = __builtin_amdgcn_permlane16_swap(__float_as_uint(x), __float_as_uint(x), false, false);
  x = __uint_as_float(s[0]) + __uint_as_float(s[1]);
  auto t = __builtin_amdgcn_permlane32_swap(__float_as_uint(x), __float_as_uint(x), false, false);
  return __uint_as_float(t[0]) + __uint_as_float(t[1]);
}
__device__ __forceinline__ float xrow_max(float x) {
  auto s = __builtin_amdgcn_permlane16_swap(__float_as_uint(x), __float_as_uint(x), false, false);
  x = fmaxf(__uint_as_float(s[0]), __uint_as_float(s[1]));
  auto t = __builtin_amdgcn_permlane32_swap(__float_as_uint(x), __float_as_uint(x), false, false);
  return fmaxf(__uint_as_float(t[0]), __uint_as_float(t[1]));
}
__device__ __forceinline__ void xrow_scatter(const float (&y)[8], float& o0, float& o1) {
  float z[4];
#pragma unroll
  for (int k = 0; k < 4; ++k) { auto t = __builtin_amdgcn_permlane32_swap(__float_as_uint(y[k]), __float_as_uint(y[k + 4]), false, false); z[k] = __uint_as_float(t[0]) + __uint_as_float(t[1]); }
  auto a = __builtin_amdgcn_permlane16_swap(__float_as_uint(z[0]), __float_as_uint(z[2]), false, false);
  auto d = __builtin_amdgcn_permlane16_swap(__float_as_uint(z[1]), __float_as_uint(z[3]), false, false);
  o0 = __uint_as_float(a[0]) + __uint_as_float(a[1]); o1 = __uint_as_float(d[0]) + __uint_as_float(d[1]);
}
__device__ __forceinline__ float quadcol_sum(float x) { x += dpp<0x124>(x); x += dpp<0x128>(x); return xrow_sum(x); }
__device__ __forceinline__ float wavesum(float x) { return xrow_sum(rowsum(x)); }
__device__ __forceinline__ unsigned cvtpk(float lo, float hi) {
  typedef __attribute__((ext_vector_type(2))) float f32x2;
  return __builtin_bit_cast(unsigned, __builtin_convertvector(f32x2{lo, hi}, bf16x2));
}
__device__ __forceinline__ float blo(unsigned u) { return __uint_as_float(u << 16); }
__device__ __forceinline__ float bhi(unsigned u) { return __uint_as_float(u & 0xffff0000u); }
__device__ __forceinline__ float rbf(float x) { return blo(cvtpk(x, x)); }
__device__ __forceinline__ float silu(float x) { return x / (1.f + __expf(-x)); }
template <int AUX> __device__ __forceinline__ u32x4 bload(rsrc_t r, int voff, int soff) {
  return __builtin_amdgcn_raw_buffer_load_b128(r, voff, soff, AUX);
}
__device__ __forceinline__ rsrc_t mkrsrc(const void* p, unsigned bytes) {
  return __builtin_amdgcn_make_buffer_rsrc((void*)p, 0, bytes, 0x00020000);
}
__device__ __forceinline__ unsigned ticks() { return (unsigned)__builtin_amdgcn_s_memrealtime(); }

__device__ __forceinline__ void layernorm8(u32x4 xr, float (&o)[8]) {
  float v[8];
#pragma unroll
  for (int k = 0; k < 4; ++k) { v[2 * k] = blo(xr[k]); v[2 * k + 1] = bhi(xr[k]); }
  float s = 0.f;
#pragma unroll
  for (int k = 0; k < 8; ++k) s += v[k];
  const float mean = wavesum(s) * (1.f / DM);
  float q = 0.f;
#pragma unroll
  for (int k = 0; k < 8; ++k) q = fmaf(v[k] - mean, v[k] - mean, q);
  const float var = wavesum(q) * (1.f / DM);
  const float mb = rbf(mean), inv = 1.f / rbf(sqrtf(rbf(rbf(var) + 1e-5f)));
#pragma unroll
  for (int k = 0; k < 8; ++k) o[k] = rbf(rbf(v[k] - mb) * inv);
}

struct Params {
  const void *x, *k, *v, *wq, *wo, *w1, *w3, *w2;
  void* out; unsigned char* ws;
};

constexpr int L_XN = 0;
constexpr int L_XQ = L_XN + 2048;
constexpr int L_QP = L_XQ + 512;
constexpr int L_GU = L_QP + 512;
constexpr int L_H = L_GU + 2048;
constexpr int L_REC = L_H + 256;
constexpr int L_S = L_REC + 4 * 132;
constexpr int L_CNT = L_S + 4096;
constexpr int L_TOT = L_CNT + 4;

__device__ __forceinline__ void helper_sync(float* lds, int idx, int lane) {
  LGKM0;
  lu32* c = (lu32*)(lds + L_CNT + idx);
  if (lane == 0) __hip_atomic_fetch_add(c, 1u, __ATOMIC_RELAXED, __HIP_MEMORY_SCOPE_WORKGROUP);
  while (__builtin_amdgcn_readfirstlane(__hip_atomic_load(c, __ATOMIC_RELAXED, __HIP_MEMORY_SCOPE_WORKGROUP)) < 4u) __builtin_amdgcn_s_sleep(1);
  asm volatile("" ::: "memory");
}

__global__ void __launch_bounds__(512, 2) fused_decode(Params p) {
  __shared__ __attribute__((aligned(16))) float lds[L_TOT];
  const unsigned t0 = ticks();
  const int tid = threadIdx.x, lane = tid & 63, wave = __builtin_amdgcn_readfirstlane(tid >> 6);
  const int r = lane >> 4, c = lane & 15;
  const int wg = blockIdx.x, j = wg >> 3;
  constexpr int hA = HMAP / 10, hB = HMAP % 10;
  const int h1 = ((wg & 7) >> hA << (hA + 1)) | (((j >> 3) & 1) << hA) | ((wg & 7) & ((1 << hA) - 1));
  const int h = (h1 >> hB << (hB + 1)) | ((j >> 4) << hB) | (h1 & ((1 << hB) - 1)), b = (j & 7) >> 1, half = j & 1;

  if (wave < NSW) {
    __builtin_amdgcn_s_setprio(PRIO);
    const size_t base = (size_t)(b * NH + h) * SK * DH * 2;
    const char *kp = (const char*)p.k + base, *vp = (const char*)p.v + base;
    const int voff = lane * 16, slot = 2 * wave + half;
    const int sx = (b >> 1) * 16 + (b & 1) * 32;
    u32x4 TB[PB], TR[RING];
    int pace = PACE;
    unsigned tz = t0;
    int late = 0;
#define WINW(i) do { if (late > LAG2) VMCNT(WIN_M - 1); else if (late > LAG) VMCNT(WIN_L - 1); else if ((i) & 1) VMCNT(WIN_B - 1); else VMCNT(WIN_A - 1); } while (0)
#define PACEW(n) do { if (PACE) { const unsigned due_ = (unsigned)(((n) * 2 + wave) * pace) >> 10; unsigned now_ = ticks() - tz; late = (int)(now_ - due_); while (now_ < due_) { __builtin_amdgcn_s_sleep(1); now_ = ticks() - tz; } } } while (0)
#define ISSUE(dst, rs, g, i) ISSUX(dst, rs, g, i, K_AUX)
#define ISSUX(dst, rs, g, i, ax) do { WINW(i); if (!((i) & 1) && (g) + (i) >= 6) PACEW((g) + (i) - 4); SB dst = bload<ax>(rs, voff, (((((g) & (NT - 1)) ^ sx) + (i)) * 8192 + (((slot + (i)) & 7) << 10))); SB } while (0)
    {
      const rsrc_t rk = mkrsrc(kp, SK * DH * 2);
#pragma unroll
      for (int s = 0; s < PB + RING; ++s) ISSUE((s < PB ? TB[s] : TR[s - PB]), rk, s & ~7, s & 7);
    }
    WGBAR;
    tz = t0 + max((int)(ticks() - t0) - T_FILL, 0); late = 0;
    u32x4 q;
    {
      f32x4 a = {0, 0, 0, 0}, d = {0, 0, 0, 0};
#pragma unroll
      for (int w = 0; w < 4; ++w) { a += *(const f32x4*)(lds + L_QP + w * 128 + 8 * c); d += *(const f32x4*)(lds + L_QP + w * 128 + 8 * c + 4); }
      q = u32x4{cvtpk(a[0], a[1]), cvtpk(a[2], a[3]), cvtpk(d[0], d[1]), cvtpk(d[2], d[3])};
    }
    float* sl = lds + L_S + wave * 1024 + lane;
    float m = -INFINITY, sa = 0.f, sb = 0.f;
#define QK(Kx, S, t) do { \
      float d = dot2(Kx[0], q[0], 0.f); d = dot2(Kx[1], q[1], d); d = dot2(Kx[2], q[2], d); d = dot2(Kx[3], q[3], d); \
      d = rowsum(d); S = c == (t) ? d : S; asm volatile("" : "+v"(S)); SB } while (0)
    {
      const rsrc_t rk = mkrsrc(kp, SK * DH * 2);
#pragma unroll
      for (int i = 0; i < PB; ++i) {
        QK(TR[i & 7], sa, i & 15); ISSUE(TR[i & 7], rk, (PB + RING + i) & ~7, i & 7); QK(TB[i], sb, i & 15);
        if ((i & 15) == 15) { sl[((PB + i) >> 4) * 64] = sa; sl[(i >> 4) * 64] = sb; m = fmaxf(m, fmaxf(sa, sb)); }
      }
      for (int g = 2 * PB; g < NT; g += 16) {
        const rsrc_t r2 = mkrsrc(g + 16 < NT ? kp : vp, SK * DH * 2);
#pragma unroll
        for (int i = 0; i < 8; ++i) { QK(TR[i], sa, i); ISSUE(TR[i], rk, g + 8, i); }
#pragma unroll
        for (int i = 0; i < 8; ++i) { QK(TR[i], sa, i + 8); ISSUE(TR[i], r2, g + 16, i); }
        sl[g * 4] = sa; m = fmaxf(m, sa);
        if (SKEW) pace = PACE + __builtin_amdgcn_readfirstlane(__hip_atomic_load((lu32*)(lds + L_CNT + 3), __ATOMIC_RELAXED, __HIP_MEMORY_SCOPE_WORKGROUP));
      }
    }
    m = fmaxf(m, dpp<0x128>(m)); m = fmaxf(m, dpp<0x124>(m)); m = fmaxf(m, dpp<0x122>(m)); m = fmaxf(m, dpp<0x121>(m));
    const float M = xrow_max(m), mc = -M * C2;
    float l = 0.f, acc[8];
#pragma unroll
    for (int k = 0; k < 8; ++k) acc[k] = 0.f;
    unsigned pg;
#define PV(Vx, t) do { const unsigned pl = bc<t>(pg), ph = pl << 16; \
      _Pragma("unroll") for (int k = 0; k < 4; ++k) { acc[2 * k] = dot2(Vx[k], pl, acc[2 * k]); acc[2 * k + 1] = dot2(Vx[k], ph, acc[2 * k + 1]); } \
      asm volatile("" : "+v"(acc[0]), "+v"(acc[1]), "+v"(acc[2]), "+v"(acc[3]), "+v"(acc[4]), "+v"(acc[5]), "+v"(acc[6]), "+v"(acc[7])); SB } while (0)
#define PV8(o, X) PV(TR[0], o); X(0) PV(TR[1], o + 1); X(1) PV(TR[2], o + 2); X(2) PV(TR[3], o + 3); X(3) PV(TR[4], o + 4); X(4) PV(TR[5], o + 5); X(5) PV(TR[6], o + 6); X(6) PV(TR[7], o + 7); X(7)
#define IS_A(i) ISSUX(TR[i], rv, NT + g + 8, i, V_AUX);
#define IS_B(i) ISSUX(TR[i], rv, NT + g + 16, i, V_AUX);
#define IS_0(i)
    {
      const rsrc_t rv = mkrsrc(vp, SK * DH * 2);
      sa = sl[0];
      for (int g = 0;; g += 16) {
        pg = cvtpk(__builtin_amdgcn_exp2f(fmaf(sa, C2, mc)), 0.f); l += blo(pg);
        sa = sl[((g + 16) & (NT - 1)) * 4];
        PV8(0, IS_A)
        if (g + 16 == NT) break;
        PV8(8, IS_B)
      }
    }
    l = wavesum(l);
    PV8(8, IS_0)
    float o0, o1;
    xrow_scatter(acc, o0, o1);
    float* rec = lds + L_REC + wave * 132;
    *(float2*)(rec + 4 + 8 * c + 2 * r) = float2{o0, o1};
    if (lane == 0) *(float2*)rec = float2{M, l};
    WGBAR;
    return;
  }

  const int hw = wave - NSW;
  const rsrc_t rws = mkrsrc(p.ws, WS_END);
  const rsrc_t rx = mkrsrc(p.x, NB * DM * 2);
  const unsigned ev = __hip_atomic_load((gu32*)(p.ws + (lane & 3) * 256), __ATOMIC_RELAXED, __HIP_MEMORY_SCOPE_AGENT);
  const u32x4 xb = bload<0>(rx, lane * 16, b * DM * 2);
  u32x4 W[32];
  {
    const rsrc_t rq = mkrsrc(p.wq, DM * NH * DH * 2);
    const int vo = (hw * 128 + r * 32) * (NH * DH * 2) + h * (DH * 2) + c * 16;
    SB
#pragma unroll
    for (int t = 0; t < WQ_WIN; ++t) { W[t] = bload<W_AUX>(rq, vo, t * (NH * DH * 2)); SB }
    if (tid == NSW * 64) { lds[L_CNT] = 0.f; lds[L_CNT + 1] = 0.f; lds[L_CNT + 2] = 0.f; lds[L_CNT + 3] = 0.f; }
    float xn[8];
    layernorm8(xb, xn);
    if (r == hw) { *(f32x4*)(lds + L_XQ + hw * 128 + 8 * c) = f32x4{xn[0], xn[1], xn[2], xn[3]}; *(f32x4*)(lds + L_XQ + hw * 128 + 8 * c + 4) = f32x4{xn[4], xn[5], xn[6], xn[7]}; }
    LGKM0;
    u32x4 xq[8];
#pragma unroll
    for (int u = 0; u < 8; ++u) xq[u] = *(const u32x4*)(lds + L_XQ + hw * 128 + r * 32 + 4 * u);
    float qa[8];
#pragma unroll
    for (int k = 0; k < 8; ++k) qa[k] = 0.f;
    SB
#pragma unroll
    for (int t = 0; t < 32; ++t) {
      if (t + WQ_WIN < 32) { VMCNT(WQ_WIN - 1); SB W[t + WQ_WIN] = bload<W_AUX>(rq, vo, (t + WQ_WIN) * (NH * DH * 2)); SB }
      const unsigned xh = xq[t >> 2][t & 3], xl = xh >> 16;
#pragma unroll
      for (int k = 0; k < 4; ++k) { qa[2 * k] = dot2(W[t][k], xl, qa[2 * k]); qa[2 * k + 1] = dot2(W[t][k], xh, qa[2 * k + 1]); }
      SB
    }
    float q0, q1;
    xrow_scatter(qa, q0, q1);
    *(float2*)(lds + L_QP + hw * 128 + 8 * c + 2 * r) = float2{q0, q1};
  }
  WGBAR;
  const unsigned e0 = __builtin_amdgcn_readlane(ev, 0) + 1, e1 = __builtin_amdgcn_readlane(ev, 1) + 1, e2 = __builtin_amdgcn_readlane(ev, 2) + 1, e3 = __builtin_amdgcn_readlane(ev, 3) + 1;
  const unsigned eb = b == 0 ? e0 : b == 1 ? e1 : b == 2 ? e2 : e3;

  if (SKEW && hw == 1) {
    const unsigned tz = t0 + max((int)(ticks() - t0) - T_FILL, 0);
    if (wg < 8 && lane == 0) __builtin_amdgcn_raw_buffer_store_b128(u32x4{tz, e0, tz, e0}, rws, 2048 + wg * 64, 0, 16);
    while (ticks() - tz < 1200) __builtin_amdgcn_s_sleep(8);
    while (true) {
      const u32x4 v = bload<16>(rws, 2048 + (lane & 7) * 64, 0);
      if (__all(v[1] == e0 && v[3] == e0)) {
        const float d = wavesum(fminf(fmaxf((float)(int)(v[0] - tz), -600.f), 600.f)) * (1.f / 64);
        if (lane == 0) __hip_atomic_store((lu32*)(lds + L_CNT + 3), (unsigned)(int)d, __ATOMIC_RELAXED, __HIP_MEMORY_SCOPE_WORKGROUP);
        break;
      }
      if (ticks() - tz > 2200) break;
      __builtin_amdgcn_s_sleep(64);
    }
  }
  if (wg < NFFN) {
    const int f = wg;
    {
      float xo[8];
      layernorm8(bload<0>(rx, lane * 16, hw * DM * 2), xo);
#pragma unroll
      for (int k = 0; k < 8; ++k) lds[L_XN + (lane * 8 + k) * 4 + hw] = xo[k];
      helper_sync(lds, 2, lane);
    }
    const rsrc_t r1 = mkrsrc(p.w1, DM * FF * 2), r3 = mkrsrc(p.w3, DM * FF * 2), r2 = mkrsrc(p.w2, FF * DM * 2);
    const int rr = lane >> 3, cc = lane & 7;
    const int vo = (hw * 128 + rr) * (FF * 2) + f * 128 + cc * 16;
    float ga[4][8], ua[4][8];
#pragma unroll
    for (int bb = 0; bb < 4; ++bb)
#pragma unroll
      for (int k = 0; k < 8; ++k) { ga[bb][k] = 0.f; ua[bb][k] = 0.f; }
    u32x4 n1 = bload<W_AUX>(r1, vo, 0), n3 = bload<W_AUX>(r3, vo, 0);
#pragma unroll 1
    for (int t = 0; t < 16; ++t) {
      const u32x4 a1 = n1, a3 = n3;
      const int tn = t < 15 ? t + 1 : 15;
      while (ticks() - t0 < (unsigned)(1000 + t * FFN_DT + hw * (FFN_DT / 4))) __builtin_amdgcn_s_sleep(8);
      n1 = bload<W_AUX>(r1, vo, tn * 8 * (FF * 2)); n3 = bload<W_AUX>(r3, vo, tn * 8 * (FF * 2));
      const f32x4 xv = *(const f32x4*)(lds + L_XN + (hw * 128 + t * 8 + rr) * 4);
#pragma unroll
      for (int k = 0; k < 4; ++k) {
        const float g0 = blo(a1[k]), g1 = bhi(a1[k]), u0 = blo(a3[k]), u1 = bhi(a3[k]);
#pragma unroll
        for (int bb = 0; bb < 4; ++bb) {
          ga[bb][2 * k] = fmaf(xv[bb], g0, ga[bb][2 * k]); ga[bb][2 * k + 1] = fmaf(xv[bb], g1, ga[bb][2 * k + 1]);
          ua[bb][2 * k] = fmaf(xv[bb], u0, ua[bb][2 * k]); ua[bb][2 * k + 1] = fmaf(xv[bb], u1, ua[bb][2 * k + 1]);
        }
      }
    }
    const int vo2 = (f * 64 + r * 16) * (DM * 2) + hw * 256 + c * 16;
#pragma unroll
    for (int bb = 0; bb < 4; ++bb) {
#pragma unroll
      for (int k = 0; k < 8; ++k) {
        float g = ga[bb][k], u = ua[bb][k];
        g += dpp<0x128>(g); u += dpp<0x128>(u);
        ga[bb][k] = xrow_sum(g); ua[bb][k] = xrow_sum(u);
      }
      if (lane < 8) {
        float* o = lds + L_GU + hw * 512 + bb * 64 + cc * 8;
        *(f32x4*)(o) = f32x4{ga[bb][0], ga[bb][1], ga[bb][2], ga[bb][3]}; *(f32x4*)(o + 4) = f32x4{ga[bb][4], ga[bb][5], ga[bb][6], ga[bb][7]};
        *(f32x4*)(o + 256) = f32x4{ua[bb][0], ua[bb][1], ua[bb][2], ua[bb][3]}; *(f32x4*)(o + 260) = f32x4{ua[bb][4], ua[bb][5], ua[bb][6], ua[bb][7]};
      }
    }
    helper_sync(lds, 0, lane);
    {
      float g = 0.f, u = 0.f;
#pragma unroll
      for (int w = 0; w < 4; ++w) { g += lds[L_GU + w * 512 + hw * 64 + lane]; u += lds[L_GU + w * 512 + 256 + hw * 64 + lane]; }
      lds[L_H + lane * 4 + hw] = rbf(rbf(silu(rbf(g))) * rbf(u));
    }
    helper_sync(lds, 1, lane);
    float oa[4][8];
#pragma unroll
    for (int bb = 0; bb < 4; ++bb)
#pragma unroll
      for (int k = 0; k < 8; ++k) oa[bb][k] = 0.f;
    n1 = bload<W_AUX>(r2, vo2, 0); n3 = bload<W_AUX>(r2, vo2, DM * 2);
#pragma unroll 1
    for (int t = 0; t < 16; t += 2) {
      const u32x4 a1 = n1, a3 = n3;
      const int tn = t < 14 ? t + 2 : 14;
      n1 = bload<W_AUX>(r2, vo2, tn * (DM * 2)); n3 = bload<W_AUX>(r2, vo2, (tn + 1) * (DM * 2));
      const f32x4 hv = *(const f32x4*)(lds + L_H + (r * 16 + t) * 4), hu = *(const f32x4*)(lds + L_H + (r * 16 + t + 1) * 4);
#pragma unroll
      for (int k = 0; k < 4; ++k) {
        const float a0 = blo(a1[k]), b0 = bhi(a1[k]), c0 = blo(a3[k]), d0 = bhi(a3[k]);
#pragma unroll
        for (int bb = 0; bb < 4; ++bb) { oa[bb][2 * k] = fmaf(hu[bb], c0, fmaf(hv[bb], a0, oa[bb][2 * k])); oa[bb][2 * k + 1] = fmaf(hu[bb], d0, fmaf(hv[bb], b0, oa[bb][2 * k + 1])); }
      }
    }
#pragma unroll
    for (int bb = 0; bb < 4; ++bb) {
      float v0, v1;
      xrow_scatter(oa[bb], v0, v1);
      const unsigned tg = bb == 0 ? e0 : bb == 1 ? e1 : bb == 2 ? e2 : e3;
      __builtin_amdgcn_raw_buffer_store_b128(u32x4{__float_as_uint(v0), tg, __float_as_uint(v1), tg}, rws, (hw * 64 + c * 4 + r) * 16 + WS_F + (f * 4 + bb) * 4096, 0, ST_AUX);
    }
  }

  {
    const int tw = max(2500, (int)(ticks() - t0)), dt = max(10, min(100, (6600 - tw) >> 5));
    const rsrc_t ro = mkrsrc(p.wo, NH * DH * DM * 2);
#pragma unroll
    for (int t = 0; t < 32; ++t) {
      while (ticks() - t0 < (unsigned)(tw + t * dt + hw * (dt >> 2))) __builtin_amdgcn_s_sleep(8);
      SB W[t] = bload<W_AUX>(ro, r * 32 * (DM * 2) + hw * 256 + c * 16, (h * DH + t) * (DM * 2)); SB
    }
#pragma unroll
    for (int t = 0; t < 32; t += 2) {
      u32x4 lo, hi;
#pragma unroll
      for (int k = 0; k < 4; ++k) { lo[k] = __builtin_amdgcn_perm(W[t + 1][k], W[t][k], 0x05040100u); hi[k] = __builtin_amdgcn_perm(W[t + 1][k], W[t][k], 0x07060302u); }
      W[t] = lo; W[t + 1] = hi;
    }
  }
  const int cg = h * 2 + half, go = cg * 64 + (lane & 3) * 16, pr = lane >> 2;
  float gate0 = 0.f, gate1 = 0.f;
  unsigned xpair = 0;
  if (hw == 0) {
    u32x4 v0 = {0, 0, 0, 0}, v1 = {0, 0, 0, 0};
    bool k0 = false, k1 = pr + 16 >= NFFN;
    while (true) {
      if (!k0) v0 = bload<16>(rws, (pr * 4 + b) * 4096 + go, WS_F);
      if (!k1) v1 = bload<16>(rws, ((pr + 16) * 4 + b) * 4096 + go, WS_F);
      k0 = v0[1] == eb && v0[3] == eb; k1 = pr + 16 >= NFFN || (v1[1] == eb && v1[3] == eb);
      if (__all(k0 && k1) || ticks() - t0 > SPIN_CAP) break;
      __builtin_amdgcn_s_sleep(64);
    }
    gate0 = rbf(silu(rbf(quadcol_sum(__uint_as_float(v0[0]) + __uint_as_float(v1[0])))));
    gate1 = rbf(silu(rbf(quadcol_sum(__uint_as_float(v0[2]) + __uint_as_float(v1[2])))));
    xpair = ((const unsigned*)p.x)[b * (DM / 2) + cg * 4 + (lane & 3)];
    VMCNT(0);
  }
  WGBAR;
  float lse, y0, y1;
  {
    const float* rec = lds + L_REC;
    const float m0 = rec[0], m1 = rec[132], m2 = rec[264], m3 = rec[396];
    const float M = fmaxf(fmaxf(m0, m1), fmaxf(m2, m3));
    const float w0 = __builtin_amdgcn_exp2f((m0 - M) * C2), w1 = __builtin_amdgcn_exp2f((m1 - M) * C2), w2 = __builtin_amdgcn_exp2f((m2 - M) * C2), w3 = __builtin_amdgcn_exp2f((m3 - M) * C2);
    const float L = w0 * rec[1] + w1 * rec[133] + w2 * rec[265] + w3 * rec[397];
    const float inv = __builtin_amdgcn_rcpf(L);
    lse = fmaf(M, C2, __builtin_amdgcn_logf(L));
    const float2 a0 = *(const float2*)(rec + 4 + 2 * lane), a1 = *(const float2*)(rec + 136 + 2 * lane), a2 = *(const float2*)(rec + 268 + 2 * lane), a3 = *(const float2*)(rec + 400 + 2 * lane);
    const unsigned op = cvtpk((w0 * a0.x + w1 * a1.x + w2 * a2.x + w3 * a3.x) * inv, (w0 * a0.y + w1 * a1.y + w2 * a2.y + w3 * a3.y) * inv);
    float y[8];
#pragma unroll
    for (int k = 0; k < 8; ++k) y[k] = 0.f;
    unsigned ob[16];
    ob[0] = bc<0>(op); ob[1] = bc<1>(op); ob[2] = bc<2>(op); ob[3] = bc<3>(op); ob[4] = bc<4>(op); ob[5] = bc<5>(op); ob[6] = bc<6>(op); ob[7] = bc<7>(op);
    ob[8] = bc<8>(op); ob[9] = bc<9>(op); ob[10] = bc<10>(op); ob[11] = bc<11>(op); ob[12] = bc<12>(op); ob[13] = bc<13>(op); ob[14] = bc<14>(op); ob[15] = bc<15>(op);
#pragma unroll
    for (int t = 0; t < 16; ++t)
#pragma unroll
      for (int k = 0; k < 4; ++k) { y[2 * k] = dot2(W[2 * t][k], ob[t], y[2 * k]); y[2 * k + 1] = dot2(W[2 * t + 1][k], ob[t], y[2 * k + 1]); }
    xrow_scatter(y, y0, y1);
  }
  const unsigned tg = eb << 16, lb = __float_as_uint(lse);
  __builtin_amdgcn_raw_buffer_store_b128(u32x4{__float_as_uint(y0), tg | (lb & 0xffffu), __float_as_uint(y1), tg | (lb >> 16)}, rws, (hw * 64 + c * 4 + r) * 16 + WS_Y + (b * 64 + cg) * 4096, 0, ST_AUX);
  if (hw != 0) return;
  u32x4 v[4];
  bool ok[4];
#pragma unroll
  for (int i = 0; i < 4; ++i) { v[i] = u32x4{0, 0, 0, 0}; ok[i] = false; }
  while (true) {
#pragma unroll
    for (int i = 0; i < 4; ++i) if (!ok[i]) v[i] = bload<16>(rws, pr * 8192 + go, WS_Y + (b * 64 + (i >> 1) * 32 + (i & 1)) * 4096);
#pragma unroll
    for (int i = 0; i < 4; ++i) ok[i] = ((v[i][1] ^ tg) | (v[i][3] ^ tg)) < 0x10000u;
    if (__all(ok[0] && ok[1] && ok[2] && ok[3]) || ticks() - t0 > SPIN_CAP) break;
    if (FIN_SLEEP) __builtin_amdgcn_s_sleep(FIN_SLEEP);
  }
  float s0 = 0.f, s1 = 0.f;
#pragma unroll
  for (int i = 0; i < 4; i += 2) {
    const float la = __uint_as_float((v[i][1] & 0xffffu) | (v[i][3] << 16)), lc = __uint_as_float((v[i + 1][1] & 0xffffu) | (v[i + 1][3] << 16));
    const float wa = __builtin_amdgcn_rcpf(1.f + __builtin_amdgcn_exp2f(lc - la)), wc = __builtin_amdgcn_rcpf(1.f + __builtin_amdgcn_exp2f(la - lc));
    s0 += wa * __uint_as_float(v[i][0]) + wc * __uint_as_float(v[i + 1][0]); s1 += wa * __uint_as_float(v[i][2]) + wc * __uint_as_float(v[i + 1][2]);
  }
  s0 = quadcol_sum(s0); s1 = quadcol_sum(s1);
  if (lane < 4) ((unsigned*)p.out)[b * (DM / 2) + cg * 4 + lane] = cvtpk(blo(xpair) + rbf(rbf(s0) * gate0), bhi(xpair) + rbf(rbf(s1) * gate1));
  if (lane == 0 && cg == 0) __hip_atomic_store((gu32*)(p.ws + b * 256), eb, __ATOMIC_RELAXED, __HIP_MEMORY_SCOPE_AGENT);
}

extern "C" void kernel_launch(void* const* d_in, const int* in_sizes, int n_in, void* d_out, int out_size, void* d_ws, size_t ws_size, hipStream_t stream) {
  Params p;
  p.x = d_in[0]; p.k = d_in[1]; p.v = d_in[2]; p.wq = d_in[3]; p.wo = d_in[4];
  p.w1 = d_in[6]; p.w3 = d_in[7]; p.w2 = d_in[8];
  p.out = d_out; p.ws = (unsigned char*)d_ws;
  fused_decode<<<256, 512, 0, stream>>>(p);
}
```
